```python
import torch

def _softmax(x, axis=-1):
    return torch.softmax(x, dim=axis)
def _gelu(x):
    return torch.nn.functional.gelu(x, approximate="tanh")
def _silu(x):
    return torch.nn.functional.silu(x)
def _sigmoid(x):
    return torch.sigmoid(x)
def _layernorm(x, axis=-1, eps=1e-5):
    m = x.mean(dim=axis, keepdim=True)
    v = x.var(dim=axis, keepdim=True, unbiased=False)
    return (x - m) / torch.sqrt(v + eps)
def _scatter_add(tgt, idx, src, axis=0):
    return tgt.clone().index_add_(axis, idx, src)
def _segment_sum(src, seg, n):
    out = torch.zeros((n,) + src.shape[1:], dtype=src.dtype)
    return out.index_add_(0, seg, src)
def _pad(x, pw):
    flat = tuple(v for pair in reversed(pw) for v in pair)
    return torch.nn.functional.pad(x, flat)

kernel_name = 'sequential_preLN__compressed_kv_cla__swiglu_ffn__decode'

def setup_inputs():

    _w = (lambda a: (lambda t: t.to(torch.bfloat16) if t.is_floating_point() else t)(torch.as_tensor(a)))
    def _A_setup():
        B, H, Sq, Sk, D, M = 4, 32, 1, 4096, 128, 512
        Dc = 4 * D
        return {
            "x": _w((lambda *s: torch.randn(*s, dtype=torch.bfloat16) * 0.05)(B, Sq, M)),
            "c_kv": _w((lambda *s: torch.randn(*s, dtype=torch.bfloat16) * 0.4)(B, Sk, Dc)),
            "w_q": _w((lambda *s: torch.randn(*s, dtype=torch.bfloat16) * 3.2)(M, H, D) * (M ** -0.5)),
            "w_uk": _w((lambda *s: torch.randn(*s, dtype=torch.bfloat16) * 3.2)(Dc, H, D) * (Dc ** -0.5)),
            "w_uv": _w((lambda *s: torch.randn(*s, dtype=torch.bfloat16) * 0.4)(Dc, H, D) * (Dc ** -0.5)),
            "w_o": _w((lambda *s: torch.randn(*s, dtype=torch.bfloat16) * 0.4)(H, D, M) * ((H * D) ** -0.5)),
        }
    _A_in = _A_setup()

    def _B_setup():
        B, S, D, F = 4, 1, 512, 1376
        return {
            "x": _w((lambda *s: torch.randn(*s, dtype=torch.bfloat16) * 0.05)(B, S, D)),
            "w_gate": _w((lambda *s: torch.randn(*s, dtype=torch.bfloat16) * 0.05)(D, F)),
            "w_up": _w((lambda *s: torch.randn(*s, dtype=torch.bfloat16) * 0.05)(D, F)),
            "w_down": _w((lambda *s: torch.randn(*s, dtype=torch.bfloat16) * 0.05)(F, D)),
        }
    _B_in = _B_setup()

    return {
        "A_x": _A_in["x"],
        "A_c_kv": _A_in["c_kv"],
        "A_w_q": _A_in["w_q"],
        "A_w_uk": _A_in["w_uk"],
        "A_w_uv": _A_in["w_uv"],
        "A_w_o": _A_in["w_o"],
        "B_x": _B_in["x"],
        "B_w_gate": _B_in["w_gate"],
        "B_w_up": _B_in["w_up"],
        "B_w_down": _B_in["w_down"],
    }

def reference(A_x, A_c_kv, A_w_q, A_w_uk, A_w_uv, A_w_o, B_x, B_w_gate, B_w_up, B_w_down):
    def _A_ref(x, c_kv, w_q, w_uk, w_uv, w_o):
        B, Sq, M = x.shape[0], x.shape[1], x.shape[2]
        Sk = c_kv.shape[1]
        H, D = w_q.shape[1], w_q.shape[2]
        q = torch.einsum("bqm,mhd->bhqd", x, w_q)
        k = torch.einsum("bkc,chd->bhkd", c_kv, w_uk)
        v = torch.einsum("bkc,chd->bhkd", c_kv, w_uv)
        scores = torch.einsum("bhqd,bhkd->bhqk", q, k) * (D ** -0.5)
        off = Sk - Sq
        qi = torch.arange(Sq)[:, None]
        kj = torch.arange(Sk)[None, :]
        cmask = kj <= (qi + off)
        scores = torch.where(cmask[None, None], scores, -1e9)
        p = _softmax(scores, axis=-1)
        ctx = torch.einsum("bhqk,bhkd->bhqd", p, v)
        return torch.einsum("bhqd,hdm->bqm", ctx, w_o)

    def _B_ref(x, w_gate, w_up, w_down):
        g = _silu(x @ w_gate)
        u = x @ w_up
        return (g * u) @ w_down

    x = A_x
    h = x + _A_ref(_layernorm(x, axis=-1), A_c_kv, A_w_q, A_w_uk, A_w_uv, A_w_o)
    h2 = h + _B_ref(_layernorm(h, axis=-1), B_w_gate, B_w_up, B_w_down)
    return h2
```

```cpp
#include <hip/hip_runtime.h>
#include <hip/hip_bf16.h>
typedef unsigned short u16;
__device__ __forceinline__ float bf2f(u16 b){ return __uint_as_float(((unsigned)b)<<16); }
__device__ __forceinline__ u16 f2bf(float f){ __hip_bfloat16 h = __float2bfloat16(f); return *(u16*)&h; }
constexpr int B=4,H=32,SK=4096,D=128,M=512,DC=512,F=1376;

__device__ float block_sum(float v, float* red){
  for(int o=32;o>0;o>>=1) v+=__shfl_xor(v,o);
  int w=threadIdx.x>>6, nw=blockDim.x>>6;
  __syncthreads();
  if((threadIdx.x&63)==0) red[w]=v;
  __syncthreads();
  float s=0; for(int i=0;i<nw;i++) s+=red[i];
  return s;
}
__global__ void k_q(const u16* x,const u16* wq,const u16* wuk,float* qabs){
  __shared__ float xn[B][M]; __shared__ float q[B][D]; __shared__ float red[8];
  int h=blockIdx.x,t=threadIdx.x;
  for(int b=0;b<B;b++){
    float s=0; for(int m=t;m<M;m+=128) s+=bf2f(x[b*M+m]);
    float mean=block_sum(s,red)/M;
    float v=0; for(int m=t;m<M;m+=128){float d=bf2f(x[b*M+m])-mean; v+=d*d;}
    float var=block_sum(v,red)/M;
    float r=1.0f/sqrtf(var+1e-5f);
    for(int m=t;m<M;m+=128) xn[b][m]=(bf2f(x[b*M+m])-mean)*r;
  }
  __syncthreads();
  float a[B]={0,0,0,0};
  for(int m=0;m<M;m++){ float w=bf2f(wq[(size_t)m*H*D+h*D+t]); for(int b=0;b<B;b++) a[b]+=xn[b][m]*w; }
  for(int b=0;b<B;b++) q[b][t]=a[b];
  __syncthreads();
  for(int c=t;c<DC;c+=128){
    float s[B]={0,0,0,0};
    for(int d=0;d<D;d++){ float w=bf2f(wuk[(size_t)c*H*D+h*D+d]); for(int b=0;b<B;b++) s[b]+=q[b][d]*w; }
    for(int b=0;b<B;b++) qabs[(b*H+h)*DC+c]=s[b]*0.08838834764831845f;
  }
}
__global__ void k_attn(const u16* ckv,const float* qabs,const u16* wuv,float* ctx){
  __shared__ float qa[DC]; __shared__ float p[SK]; __shared__ float red[8]; __shared__ float cl[DC];
  int b=blockIdx.x/H,h=blockIdx.x%H,t=threadIdx.x;
  for(int c=t;c<DC;c+=256) qa[c]=qabs[(b*H+h)*DC+c];
  __syncthreads();
  float mx=-1e30f;
  for(int k=t;k<SK;k+=256){ const u16* r=ckv+((size_t)b*SK+k)*DC; float s=0; for(int c=0;c<DC;c++) s+=qa[c]*bf2f(r[c]); p[k]=s; mx=fmaxf(mx,s);}
  for(int o=32;o>0;o>>=1) mx=fmaxf(mx,__shfl_xor(mx,o));
  __syncthreads(); if((t&63)==0) red[t>>6]=mx; __syncthreads();
  mx=fmaxf(fmaxf(red[0],red[1]),fmaxf(red[2],red[3]));
  float l=0; for(int k=t;k<SK;k+=256){ float e=__expf(p[k]-mx); p[k]=e; l+=e; }
  l=block_sum(l,red);
  for(int c=t;c<DC;c+=256){ float s=0; for(int k=0;k<SK;k++) s+=p[k]*bf2f(ckv[((size_t)b*SK+k)*DC+c]); cl[c]=s/l; }
  __syncthreads();
  if(t<D){ float s=0; for(int c=0;c<DC;c++) s+=cl[c]*bf2f(wuv[(size_t)c*H*D+h*D+t]); ctx[(b*H+h)*D+t]=s; }
}
__global__ void k_o(const u16* x,const float* ctx,const u16* wo,float* hbuf,float* hn){
  __shared__ float red[8];
  int b=blockIdx.x,t=threadIdx.x;
  float s=0; for(int i=0;i<H*D;i++) s+=ctx[b*H*D+i]*bf2f(wo[(size_t)i*M+t]);
  float hv=bf2f(x[b*M+t])+s;
  float mean=block_sum(hv,red)/M; float d=hv-mean; float var=block_sum(d*d,red)/M;
  hbuf[b*M+t]=hv; hn[b*M+t]=d/sqrtf(var+1e-5f);
}
__global__ void k_gu(const float* hn,const u16* wg,const u16* wu,float* act){
  int f=blockIdx.x*64+threadIdx.x; int b=blockIdx.y; if(f>=F) return;
  float g=0,u=0; for(int m=0;m<M;m++){ float v=hn[b*M+m]; g+=v*bf2f(wg[(size_t)m*F+f]); u+=v*bf2f(wu[(size_t)m*F+f]); }
  act[b*F+f]=g/(1.0f+__expf(-g))*u;
}
__global__ void k_dn(const float* act,const u16* wd,const float* hbuf,u16* out){
  int m=blockIdx.x*64+threadIdx.x; int b=blockIdx.y;
  float s=0; for(int f=0;f<F;f++) s+=act[b*F+f]*bf2f(wd[(size_t)f*M+m]);
  out[b*M+m]=f2bf(hbuf[b*M+m]+s);
}
extern "C" void kernel_launch(void* const* d_in, const int* in_sizes, int n_in, void* d_out, int out_size, void* d_ws, size_t ws_size, hipStream_t stream) {
  const u16* x=(const u16*)d_in[0]; const u16* ckv=(const u16*)d_in[1]; const u16* wq=(const u16*)d_in[2]; const u16* wuk=(const u16*)d_in[3];
  const u16* wuv=(const u16*)d_in[4]; const u16* wo=(const u16*)d_in[5]; const u16* wg=(const u16*)d_in[7]; const u16* wu=(const u16*)d_in[8]; const u16* wd=(const u16*)d_in[9];
  float* ws=(float*)d_ws; float* qabs=ws; float* ctx=qabs+B*H*DC; float* hbuf=ctx+B*H*D; float* hn=hbuf+B*M; float* act=hn+B*M;
  k_q<<<H,128,0,stream>>>(x,wq,wuk,qabs);
  k_attn<<<B*H,256,0,stream>>>(ckv,qabs,wuv,ctx);
  k_o<<<B,512,0,stream>>>(x,ctx,wo,hbuf,hn);
  k_gu<<<dim3((F+63)/64,B),64,0,stream>>>(hn,wg,wu,act);
  k_dn<<<dim3(M/64,B),64,0,stream>>>(act,wd,hbuf,(u16*)d_out);
}
```

```cpp
#include <hip/hip_runtime.h>
#include <hip/hip_bf16.h>

#define STAMP 0

#define GAS __attribute__((address_space(1)))
#define LAS __attribute__((address_space(3)))
#define DI __device__ __forceinline__
typedef unsigned short u16;
typedef unsigned v4u __attribute__((ext_vector_type(4)));
typedef unsigned v2u __attribute__((ext_vector_type(2)));
typedef float f32x2 __attribute__((ext_vector_type(2)));
typedef float f32x4 __attribute__((ext_vector_type(4)));
typedef float f32x16 __attribute__((ext_vector_type(16)));
typedef short bf16x8 __attribute__((ext_vector_type(8)));
typedef short s16x4 __attribute__((ext_vector_type(4)));
typedef __bf16 bf16x2 __attribute__((ext_vector_type(2)));
typedef __amdgpu_buffer_rsrc_t rsrc_t;

constexpr int NB = 4, NH = 32, SK = 4096, HD = 128, DM = 512, DC = 512, FF = 1376;
constexpr int FSW = 48, NFS = 29;

constexpr unsigned OFF_EPOCH = 0;
constexpr unsigned OFF_QA = 4096;
constexpr unsigned OFF_STAT = OFF_QA + 4 * 65536;
constexpr unsigned OFF_OP = OFF_STAT + 32768;
constexpr unsigned OFF_FP = OFF_OP + 131072;
constexpr unsigned OFF_PART = 1u << 20;
constexpr unsigned OFF_DBG = 6u << 20;
constexpr unsigned WS_BYTES = 8u << 20;

constexpr int LDS_BYTES = 163840;
constexpr int L_QA = 131072;
constexpr int L_MX = L_QA, L_LS = L_QA + 512, L_P = L_QA + 1024, PSTR = 272;
constexpr int L_HB = L_QA + 16384, L_ST = L_QA + 18432, L_RED = L_QA + 18688, L_ACT = L_QA + 20736;
constexpr int AUX_SC1 = 16;
constexpr unsigned SENT = 0xFFFFFFFFu;
constexpr int SPIN_CAP = 200000;
#define PSLEEP 4

DI float asf(unsigned u) { return __builtin_bit_cast(float, u); }
DI unsigned asu(float f) { return __builtin_bit_cast(unsigned, f); }
DI float bflo(unsigned u) { return __builtin_bit_cast(float, u << 16); }
DI float bfhi(unsigned u) { return __builtin_bit_cast(float, u & 0xFFFF0000u); }
DI unsigned pk2(float a, float b) {
  f32x2 v = {a, b};
  return __builtin_bit_cast(unsigned, __builtin_convertvector(v, bf16x2));
}
DI float rbf(float a) { return bflo(pk2(a, a) & 0xFFFFu); }
DI unsigned nosent(unsigned u) { return u < 0xFFFFFFFEu ? u : 0xFFFFFFFEu; }
DI f32x4 mfma16(v4u a, v4u b, f32x4 c) {
  return __builtin_amdgcn_mfma_f32_16x16x32_bf16(__builtin_bit_cast(bf16x8, a), __builtin_bit_cast(bf16x8, b), c, 0, 0, 0);
}
DI f32x16 mfma32(v4u a, v4u b, f32x16 c) {
  return __builtin_amdgcn_mfma_f32_32x32x16_bf16(__builtin_bit_cast(bf16x8, a), __builtin_bit_cast(bf16x8, b), c, 0, 0, 0);
}
template <int N> DI float ror16(float v) {
  return __builtin_bit_cast(float, __builtin_amdgcn_update_dpp(0, __builtin_bit_cast(int, v), 0x120 + N, 0xf, 0xf, false));
}
DI float sum16(float v) { v += ror16<8>(v); v += ror16<4>(v); v += ror16<2>(v); v += ror16<1>(v); return v; }
DI float max16(float v) {
  v = fmaxf(v, ror16<8>(v)); v = fmaxf(v, ror16<4>(v)); v = fmaxf(v, ror16<2>(v)); v = fmaxf(v, ror16<1>(v)); return v;
}
DI float swap32_add(float a, float b) {
  auto r = __builtin_amdgcn_permlane32_swap(__builtin_bit_cast(unsigned, a), __builtin_bit_cast(unsigned, b), false, false);
  return __builtin_bit_cast(float, (unsigned)r[0]) + __builtin_bit_cast(float, (unsigned)r[1]);
}
DI float swap16_add(float a, float b) {
  auto r = __builtin_amdgcn_permlane16_swap(__builtin_bit_cast(unsigned, a), __builtin_bit_cast(unsigned, b), false, false);
  return __builtin_bit_cast(float, (unsigned)r[0]) + __builtin_bit_cast(float, (unsigned)r[1]);
}
DI float swap32_max(float a) {
  auto r = __builtin_amdgcn_permlane32_swap(__builtin_bit_cast(unsigned, a), __builtin_bit_cast(unsigned, a), false, false);
  return fmaxf(__builtin_bit_cast(float, (unsigned)r[0]), __builtin_bit_cast(float, (unsigned)r[1]));
}
DI float swap16_max(float a) {
  auto r = __builtin_amdgcn_permlane16_swap(__builtin_bit_cast(unsigned, a), __builtin_bit_cast(unsigned, a), false, false);
  return fmaxf(__builtin_bit_cast(float, (unsigned)r[0]), __builtin_bit_cast(float, (unsigned)r[1]));
}
DI unsigned plo(unsigned a, unsigned b) { return __builtin_amdgcn_perm(b, a, 0x05040100u); }
DI unsigned phi(unsigned a, unsigned b) { return __builtin_amdgcn_perm(b, a, 0x07060302u); }
DI void tr8x8(v4u* R) {
  v4u F[8];
#pragma unroll
  for (int t = 0; t < 8; ++t)
#pragma unroll
    for (int p = 0; p < 4; ++p) F[t][p] = (t & 1) ? phi(R[2 * p][t >> 1], R[2 * p + 1][t >> 1]) : plo(R[2 * p][t >> 1], R[2 * p + 1][t >> 1]);
#pragma unroll
  for (int t = 0; t < 8; ++t) R[t] = F[t];
}
DI void tr8x4(const v2u* R, v4u* F) {
#pragma unroll
  for (int t = 0; t < 4; ++t)
#pragma unroll
    for (int p = 0; p < 4; ++p) F[t][p] = (t & 1) ? phi(R[2 * p][t >> 1], R[2 * p + 1][t >> 1]) : plo(R[2 * p][t >> 1], R[2 * p + 1][t >> 1]);
}
#define BAR() do { asm volatile("s_waitcnt lgkmcnt(0)" ::: "memory"); __builtin_amdgcn_s_barrier(); asm volatile("" ::: "memory"); } while (0)
#define DRAIN() asm volatile("s_waitcnt vmcnt(0)" ::: "memory")
#define SCHED() __builtin_amdgcn_sched_barrier(0)
DI v4u ldg16(const u16* p) { return *(const GAS v4u*)p; }
DI v2u ldg8(const u16* p) { return *(const GAS v2u*)p; }
DI v4u ldc16(rsrc_t rs, unsigned off) { return __builtin_amdgcn_raw_buffer_load_b128(rs, off, 0, AUX_SC1); }
DI v2u ldc8(rsrc_t rs, unsigned off) { return __builtin_amdgcn_raw_buffer_load_b64(rs, off, 0, AUX_SC1); }
DI unsigned ldc4(rsrc_t rs, unsigned off) { return __builtin_amdgcn_raw_buffer_load_b32(rs, off, 0, AUX_SC1); }
DI void stc16(rsrc_t rs, unsigned off, v4u v) { __builtin_amdgcn_raw_buffer_store_b128(v, rs, off, 0, AUX_SC1); }
DI void stc8(rsrc_t rs, unsigned off, v2u v) { __builtin_amdgcn_raw_buffer_store_b64(v, rs, off, 0, AUX_SC1); }
DI void stc4(rsrc_t rs, unsigned off, unsigned v) { __builtin_amdgcn_raw_buffer_store_b32(v, rs, off, 0, AUX_SC1); }
DI unsigned umax(unsigned a, unsigned b) { return a > b ? a : b; }
DI unsigned mx4(v4u v) { return umax(umax(v.x, v.y), umax(v.z, v.w)); }

#define TS(i) do {} while (0)

__global__ void __launch_bounds__(512, 2)
mega(const u16* __restrict__ x, const u16* __restrict__ ckv, const u16* __restrict__ wq, const u16* __restrict__ wuk,
     const u16* __restrict__ wuv, const u16* __restrict__ wo, const u16* __restrict__ wg, const u16* __restrict__ wu,
     const u16* __restrict__ wd, u16* __restrict__ out, unsigned char* ws) {
  extern __shared__ __attribute__((aligned(16))) unsigned char smem[];
  LAS unsigned char* L = (LAS unsigned char*)smem;
  const int tid = threadIdx.x, lane = tid & 63, w = __builtin_amdgcn_readfirstlane(tid >> 6);
  const int bid = blockIdx.x, xcd = bid & 7, idx = bid >> 3;
  const int q4 = lane >> 4, g = lane & 15;
  const rsrc_t rs = __builtin_amdgcn_make_buffer_rsrc((void*)ws, 0, (int)WS_BYTES, 0x00020000);
  const v4u sent4 = {SENT, SENT, SENT, SENT};
  TS(0);

  if (idx < 16) {
    const int h = xcd * 4 + (idx >> 2), j = idx & 3;
    const unsigned tag = ldc4(rs, OFF_EPOCH + (lane & 3) * 256) + 1u;
    v4u xs[4], xf[2], wr[16], wk[4];
#pragma unroll
    for (int i = 0; i < 4; ++i) xs[i] = ldg16(x + q4 * DM + g * 32 + i * 8);
#pragma unroll
    for (int ks = 0; ks < 2; ++ks) xf[ks] = ldg16(x + (lane & 3) * DM + 64 * w + 32 * ks + 8 * q4);
#pragma unroll
    for (int i = 0; i < 16; ++i) wr[i] = ldg16(wq + (size_t)(64 * w + 32 * (i >> 3) + 8 * q4 + (i & 7)) * (NH * HD) + h * HD + 8 * g);
#pragma unroll
    for (int ks = 0; ks < 4; ++ks) wk[ks] = ldg16(wuk + (size_t)(128 * j + 16 * w + g) * (NH * HD) + h * HD + 32 * ks + 8 * q4);
#pragma unroll
    for (int i = 0; i < 4; ++i) stc16(rs, OFF_PART + (j * NH + h) * 32768 + i * 8192 + tid * 16, sent4);
    if (tid < 16) stc16(rs, OFF_STAT + (j * NH + h) * 256 + tid * 16, sent4);
    if (w == 1) stc16(rs, OFF_OP + (j * NH + h) * 1024 + lane * 16, sent4);
    SCHED();
    float s1 = 0.f;
#pragma unroll
    for (int i = 0; i < 4; ++i)
#pragma unroll
      for (int e = 0; e < 4; ++e) s1 += bflo(xs[i][e]) + bfhi(xs[i][e]);
    const float mean = sum16(s1) * (1.0f / DM);
    float s2 = 0.f;
#pragma unroll
    for (int i = 0; i < 4; ++i)
#pragma unroll
      for (int e = 0; e < 4; ++e) {
        float a = bflo(xs[i][e]) - mean, b = bfhi(xs[i][e]) - mean;
        s2 += a * a + b * b;
      }
    const float var = rbf(sum16(s2) * (1.0f / DM));
    const float sd = rbf(__builtin_sqrtf(rbf(var + 1e-5f)));
    const float mb = __shfl(rbf(mean), (lane & 3) * 16), ri = __shfl(1.0f / sd, (lane & 3) * 16);
#pragma unroll
    for (int ks = 0; ks < 2; ++ks)
#pragma unroll
      for (int e = 0; e < 4; ++e) xf[ks][e] = pk2(rbf(bflo(xf[ks][e]) - mb) * ri, rbf(bfhi(xf[ks][e]) - mb) * ri);
    {
      f32x4 acc[8];
#pragma unroll
      for (int t = 0; t < 8; ++t) acc[t] = f32x4{0.f, 0.f, 0.f, 0.f};
#pragma unroll
      for (int ks = 0; ks < 2; ++ks) {
        tr8x8(wr + 8 * ks);
#pragma unroll
        for (int t = 0; t < 8; ++t) acc[t] = mfma16(xf[ks], wr[8 * ks + t], acc[t]);
      }
      TS(1);
      if (q4 == 0) {
#pragma unroll
        for (int b = 0; b < 4; ++b) {
          *(LAS f32x4*)(L + (w * 512 + b * 128 + 8 * g) * 4) = f32x4{acc[0][b], acc[1][b], acc[2][b], acc[3][b]};
          *(LAS f32x4*)(L + (w * 512 + b * 128 + 8 * g + 4) * 4) = f32x4{acc[4][b], acc[5][b], acc[6][b], acc[7][b]};
        }
      }
    }
    DRAIN();
    BAR();
    {
      float s = 0.f;
#pragma unroll
      for (int i = 0; i < 8; ++i) s += *(LAS float*)(L + (i * 512 + tid) * 4);
      *(LAS u16*)(L + 16384 + tid * 2) = (u16)(pk2(s, s) & 0xFFFFu);
    }
    BAR();
    {
      f32x4 acc = {0.f, 0.f, 0.f, 0.f};
#pragma unroll
      for (int ks = 0; ks < 4; ++ks) {
        const v4u qf = *(LAS v4u*)(L + 16384 + ((lane & 3) * 128 + 32 * ks + 8 * q4) * 2);
        acc = mfma16(wk[ks], qf, acc);
      }
      const float sc = 0.08838834764831845f * 1.4426950408889634f;
      if (g < 4) {
        v4u o = {pk2(acc[0] * sc, acc[1] * sc), tag, pk2(acc[2] * sc, acc[3] * sc), tag};
        stc16(rs, OFF_QA + g * 65536 + (8 * j + w) * 2048 + ((q4 >> 1) * 32 + h) * 32 + (q4 & 1) * 16, o);
      }
    }
    TS(2);
    v2u wor[32];
#pragma unroll
    for (int i = 0; i < 16; ++i) wr[i] = ldg16(wuv + (size_t)(64 * w + 32 * (i >> 3) + 8 * q4 + (i & 7)) * (NH * HD) + h * HD + 8 * g);
#pragma unroll
    for (int i = 0; i < 32; ++i) wor[i] = ldg8(wo + (size_t)(h * HD + 32 * (i >> 3) + 8 * q4 + (i & 7)) * DM + 64 * w + 4 * g);
    SCHED();
    tr8x8(wr);
    tr8x8(wr + 8);
    v4u wof[16];
#pragma unroll
    for (int ks = 0; ks < 4; ++ks) tr8x4(wor + 8 * ks, wof + 4 * ks);
#pragma unroll
    for (int i = 0; i < 16; ++i) asm volatile("" : "+v"(wr[i]));
#pragma unroll
    for (int i = 0; i < 16; ++i) asm volatile("" : "+v"(wof[i]));
    TS(3);
    v4u pa[4];
    v2u st;
    {
      const unsigned pbase = OFF_PART + (j * NH + h) * 32768 + g * 1024 + (64 * w + 8 * q4) * 2;
      const unsigned sbase = OFF_STAT + (j * NH + h) * 256 + (lane & 31) * 8;
#pragma nounroll
      for (int spin = 0; spin < SPIN_CAP; ++spin) {
        asm volatile("" ::: "memory");
        st = ldc8(rs, sbase);
        if (__all(umax(st.x, st.y) != SENT)) break;
        __builtin_amdgcn_s_sleep(PSLEEP);
      }
      #pragma nounroll
      for (int spin = 0; spin < SPIN_CAP; ++spin) {
        asm volatile("" ::: "memory");
        st = ldc8(rs, sbase);
#pragma unroll
        for (int i = 0; i < 4; ++i) pa[i] = ldc16(rs, pbase + (i >> 1) * 16384 + (i & 1) * 64);
        const bool ok = umax(umax(umax(st.x, st.y), umax(mx4(pa[0]), mx4(pa[1]))), umax(mx4(pa[2]), mx4(pa[3]))) != SENT;
        if (__all(ok)) break;
        __builtin_amdgcn_s_sleep(2);
      }
    }
    TS(4);
    {
      f32x4 acc[2][8];
#pragma unroll
      for (int mt = 0; mt < 2; ++mt)
#pragma unroll
        for (int t = 0; t < 8; ++t) {
          acc[mt][t] = mfma16(pa[2 * mt], wr[t], f32x4{0.f, 0.f, 0.f, 0.f});
          acc[mt][t] = mfma16(pa[2 * mt + 1], wr[8 + t], acc[mt][t]);
        }
      const float m = asf(st.x), l = asf(st.y);
      const float M = swap16_max(max16(m));
      const float e = __builtin_amdgcn_exp2f(m - M) * l;
      float tot = sum16(e);
      tot = swap16_add(tot, tot);
      LAS float* wl = (LAS float*)(L + 20480 + w * 128);
      if (lane < 32) wl[lane] = e / tot;
      const f32x4 w0 = *(LAS f32x4*)(wl + 4 * q4), w1 = *(LAS f32x4*)(wl + 16 + 4 * q4);
      float v[8];
#pragma unroll
      for (int t = 0; t < 8; ++t) {
        float a = 0.f;
#pragma unroll
        for (int r = 0; r < 4; ++r) a += w0[r] * acc[0][t][r] + w1[r] * acc[1][t][r];
        v[t] = a;
      }
#pragma unroll
      for (int t = 0; t < 4; ++t) v[t] = swap32_add(v[t], v[t + 4]);
#pragma unroll
      for (int t = 0; t < 2; ++t) v[t] = swap16_add(v[t], v[t + 2]);
      *(LAS f32x2*)(L + (w * 128 + 8 * g + 2 * q4) * 4) = f32x2{v[0], v[1]};
    }
    BAR();
    {
      f32x2 s = {0.f, 0.f};
#pragma unroll
      for (int i = 0; i < 8; ++i) s += *(LAS f32x2*)(L + (i * 128 + 2 * lane) * 4);
      LAS unsigned char* cl = L + 8192 + w * 256;
      *(LAS unsigned*)(cl + lane * 4) = pk2(s[0], s[1]);
      f32x4 acc[4];
#pragma unroll
      for (int t = 0; t < 4; ++t) acc[t] = f32x4{0.f, 0.f, 0.f, 0.f};
#pragma unroll
      for (int ks = 0; ks < 4; ++ks) {
        const v4u cf = *(LAS v4u*)(cl + (32 * ks + 8 * q4) * 2);
#pragma unroll
        for (int t = 0; t < 4; ++t) acc[t] = mfma16(cf, wof[4 * ks + t], acc[t]);
      }
      if (q4 == 0) {
        v2u o = {nosent(pk2(acc[0][0], acc[1][0])), nosent(pk2(acc[2][0], acc[3][0]))};
        stc8(rs, OFF_OP + (j * NH + h) * 1024 + (64 * w + 4 * g) * 2, o);
      }
    }
    TS(5);
    return;
  }

  const int b = idx & 3, s = xcd * 4 + ((idx - 16) >> 2);
  const bool ffn = s < NFS;
  if (w < 4) {
    const u16* src = ckv + ((size_t)b * SK + s * 128 + 32 * w) * DC;
#pragma unroll
    for (int i = 0; i < 32; ++i) {
      const int f = ((i & 3) << 2) | ((i >> 2) & 3);
      __builtin_amdgcn_global_load_lds((const GAS void*)(src + i * DC + (lane ^ f) * 8), (LAS void*)(L + (32 * w + i) * 1024), 16, 0, 0);
    }
    DRAIN();
    TS(1);
    BAR();
    TS(2);
    const int r32 = lane & 31, hf = lane >> 5;
    f32x16 sa;
#pragma unroll
    for (int i = 0; i < 16; ++i) sa[i] = 0.f;
    {
      const int f = ((r32 & 3) << 2) | ((r32 >> 2) & 3);
#pragma unroll
      for (int ks = 0; ks < 32; ++ks) {
        const v4u a = *(LAS v4u*)(L + (32 * w + r32) * 1024 + 16 * ((2 * ks + hf) ^ f));
        const v4u bq = *(LAS v4u*)(L + L_QA + ks * 1024 + lane * 16);
        sa = mfma32(a, bq, sa);
      }
    }
    float mx = sa[0];
#pragma unroll
    for (int i = 1; i < 16; ++i) mx = fmaxf(mx, sa[i]);
    mx = swap32_max(mx);
    BAR();
    *(LAS float*)(L + L_MX + (w * 32 + r32) * 4) = mx;
    BAR();
#pragma unroll
    for (int i = 0; i < 4; ++i) mx = fmaxf(mx, *(LAS float*)(L + L_MX + (i * 32 + r32) * 4));
    float ls = 0.f;
#pragma unroll
    for (int gq = 0; gq < 4; ++gq) {
      float p[4];
#pragma unroll
      for (int e = 0; e < 4; ++e) { p[e] = __builtin_amdgcn_exp2f(sa[4 * gq + e] - mx); ls += p[e]; }
      *(LAS v2u*)(L + L_P + r32 * PSTR + (32 * w + 8 * gq + 4 * hf) * 2) = v2u{pk2(p[0], p[1]), pk2(p[2], p[3])};
    }
    ls = swap32_add(ls, ls);
    *(LAS float*)(L + L_LS + (w * 32 + r32) * 4) = ls;
    BAR();
    TS(3);
    float lt = 0.f;
#pragma unroll
    for (int i = 0; i < 4; ++i) lt += *(LAS float*)(L + L_LS + (i * 32 + r32) * 4);
    if (w == 0 && lane < 32)
      stc8(rs, OFF_STAT + ((b * NH + lane) * 32 + s) * 8, v2u{nosent(__builtin_bit_cast(unsigned, mx)), nosent(__builtin_bit_cast(unsigned, lt))});
    v4u pf[8];
#pragma unroll
    for (int ks = 0; ks < 8; ++ks) pf[ks] = *(LAS v4u*)(L + L_P + r32 * PSTR + (16 * ks + 8 * hf) * 2);
    const float il = 1.0f / lt;
    f32x16 oa[4];
    {
      const int blk = (lane >> 4) & 1, qq = (lane & 15) >> 2, p = lane & 3;
#pragma unroll
      for (int ct = 0; ct < 4; ++ct) {
#pragma unroll
        for (int i = 0; i < 16; ++i) oa[ct][i] = 0.f;
#pragma unroll
        for (int ks = 0; ks < 8; ++ks) {
          s16x4 t2[2];
#pragma unroll
          for (int t = 0; t < 2; ++t) {
            const int row = 16 * ks + 8 * hf + 4 * t + qq;
            const int f = ((row & 3) << 2) | ((row >> 2) & 3);
            const int ch = 16 * w + 4 * ct + 2 * blk + (p >> 1);
            t2[t] = __builtin_amdgcn_ds_read_tr16_b64_v4i16((LAS s16x4*)(L + row * 1024 + 16 * (ch ^ f) + 8 * (p & 1)));
          }
          const bf16x8 a = __builtin_shufflevector(t2[0], t2[1], 0, 1, 2, 3, 4, 5, 6, 7);
          oa[ct] = mfma32(__builtin_bit_cast(v4u, a), pf[ks], oa[ct]);
        }
      }
    }
#pragma unroll
    for (int ct = 0; ct < 4; ++ct)
#pragma unroll
      for (int gq = 0; gq < 4; ++gq)
        *(LAS v2u*)(L + r32 * 1024 + 256 * w + 16 * ((4 * ct + gq) ^ (r32 & 15)) + 8 * hf) =
            v2u{nosent(pk2(oa[ct][4 * gq] * il, oa[ct][4 * gq + 1] * il)), nosent(pk2(oa[ct][4 * gq + 2] * il, oa[ct][4 * gq + 3] * il))};
#pragma unroll
    for (int i = 0; i < 8; ++i) {
      const int row = 4 * i + q4;
      const v4u v = *(LAS v4u*)(L + row * 1024 + 256 * w + 16 * (g ^ (row & 15)));
      stc16(rs, OFF_PART + ((b * NH + row) * 32 + s) * 1024 + 256 * w + 16 * g, v);
    }
    TS(4);
    if (!ffn) return;
  } else {
    const unsigned tag = ldc4(rs, OFF_EPOCH + b * 256) + 1u;
    if (ffn && w < 6) stc16(rs, OFF_FP + (b * 32 + s) * 2048 + (w - 4) * 1024 + lane * 16, sent4);
    {
      v4u qa[16];
      const unsigned base = OFF_QA + b * 65536 + (w - 4) * 16384 + lane * 16;
#pragma nounroll
      for (int spin = 0; spin < SPIN_CAP; ++spin) {
        asm volatile("" ::: "memory");
        const v4u t = ldc16(rs, base + 15 * 1024);
        if (__all(((t.y ^ tag) | (t.w ^ tag)) == 0)) break;
        __builtin_amdgcn_s_sleep(PSLEEP);
      }
      #pragma nounroll
      for (int spin = 0; spin < SPIN_CAP; ++spin) {
        asm volatile("" ::: "memory");
        unsigned bad = 0;
#pragma unroll
        for (int i = 0; i < 16; ++i) qa[i] = ldc16(rs, base + i * 1024);
#pragma unroll
        for (int i = 0; i < 16; ++i) bad |= (qa[i].y ^ tag) | (qa[i].w ^ tag);
        if (__all(bad == 0)) break;
        __builtin_amdgcn_s_sleep(2);
      }
#pragma unroll
      for (int i = 0; i < 16; ++i) *(LAS v2u*)(L + L_QA + (w - 4) * 8192 + i * 512 + lane * 8) = v2u{qa[i].x, qa[i].z};
    }
    DRAIN();
    TS(1);
    BAR();
    if (!ffn) { BAR(); BAR(); BAR(); return; }
  }

  v4u gw[32], dw[16];
  const int w4 = w - 4;
  const int ncol = (s == NFS - 1) ? (FF - FSW * (NFS - 1)) : FSW;
  if (w >= 4) {
    const int cg = g < 6 ? g : (g < 12 ? g - 6 : 0);
    const int col = FSW * s + ((8 * cg < ncol) ? 8 * cg : 0);
    const u16* wsel = (g >= 6 && g < 12) ? wu : wg;
#pragma unroll
    for (int i = 0; i < 32; ++i) gw[i] = ldg16(wsel + (size_t)(128 * w4 + 32 * (i >> 3) + 8 * q4 + (i & 7)) * FF + col);
#pragma unroll
    for (int i = 0; i < 16; ++i) {
      const int k = 32 * (i >> 3) + 8 * q4 + (i & 7);
      dw[i] = ldg16(wd + (size_t)(FSW * s + (k < ncol ? k : 0)) * DM + 128 * w4 + 8 * g);
    }
    SCHED();
    BAR(); BAR(); BAR();
#pragma unroll
    for (int i = 0; i < 4; ++i) tr8x8(gw + 8 * i);
    tr8x8(dw);
    tr8x8(dw + 8);
#pragma unroll
    for (int i = 0; i < 32; ++i) asm volatile("" : "+v"(gw[i]));
#pragma unroll
    for (int i = 0; i < 16; ++i) asm volatile("" : "+v"(dw[i]));
    TS(4);
  }
  {
    const int ck = lane & 7, rg = lane >> 3;
    const v4u xv = ldg16(x + b * DM + 64 * w + 8 * ck);
    v4u op[4];
    const unsigned base = OFF_OP + (b * NH + rg) * 1024 + (64 * w + 8 * ck) * 2;
#pragma nounroll
    for (int spin = 0; spin < SPIN_CAP; ++spin) {
      asm volatile("" ::: "memory");
      const v4u t = ldc16(rs, base);
      if (__all(mx4(t) != SENT)) break;
      __builtin_amdgcn_s_sleep(PSLEEP);
    }
    #pragma nounroll
      for (int spin = 0; spin < SPIN_CAP; ++spin) {
        asm volatile("" ::: "memory");
#pragma unroll
      for (int i = 0; i < 4; ++i) op[i] = ldc16(rs, base + i * 8192);
      const bool ok = umax(umax(mx4(op[0]), mx4(op[1])), umax(mx4(op[2]), mx4(op[3]))) != SENT;
      if (__all(ok)) break;
      __builtin_amdgcn_s_sleep(2);
    }
    TS(5);
    float v[8];
#pragma unroll
    for (int e = 0; e < 4; ++e) {
      v[2 * e] = (bflo(op[0][e]) + bflo(op[1][e])) + (bflo(op[2][e]) + bflo(op[3][e]));
      v[2 * e + 1] = (bfhi(op[0][e]) + bfhi(op[1][e])) + (bfhi(op[2][e]) + bfhi(op[3][e]));
    }
#pragma unroll
    for (int t = 0; t < 4; ++t) v[t] = swap32_add(v[t], v[t + 4]);
#pragma unroll
    for (int t = 0; t < 2; ++t) v[t] = swap16_add(v[t], v[t + 2]);
    v[0] += ror16<8>(v[0]);
    v[1] += ror16<8>(v[1]);
    const unsigned xw = q4 == 0 ? xv.x : (q4 == 1 ? xv.y : (q4 == 2 ? xv.z : xv.w));
    const float h0 = rbf(bflo(xw) + rbf(v[0])), h1 = rbf(bfhi(xw) + rbf(v[1]));
    *(LAS f32x2*)(L + L_HB + (64 * w + 8 * ck + 2 * q4) * 4) = f32x2{h0, h1};
    float sm = sum16(h0 + h1);
    sm = swap16_add(sm, sm);
    sm = swap32_add(sm, sm);
    const float mw = sm * (1.0f / 128.0f);
    float m2 = sum16((h0 - mw) * (h0 - mw) + (h1 - mw) * (h1 - mw));
    m2 = swap16_add(m2, m2);
    m2 = swap32_add(m2, m2);
    if (lane == 0) *(LAS f32x2*)(L + L_ST + w * 8) = f32x2{mw, m2 * 0.5f};
  }
  BAR();
  if (w >= 4) {
    float mean = 0.f, m2 = 0.f;
    f32x2 stw[8];
#pragma unroll
    for (int i = 0; i < 8; ++i) { stw[i] = *(LAS f32x2*)(L + L_ST + i * 8); mean += stw[i][0]; }
    mean *= 0.125f;
#pragma unroll
    for (int i = 0; i < 8; ++i) m2 += stw[i][1] + 64.0f * (stw[i][0] - mean) * (stw[i][0] - mean);
    const float var = rbf(m2 * (1.0f / DM));
    const float ri = 1.0f / rbf(__builtin_sqrtf(rbf(var + 1e-5f)));
    const float mb = rbf(mean);
    f32x4 acc[8];
#pragma unroll
    for (int t = 0; t < 8; ++t) acc[t] = f32x4{0.f, 0.f, 0.f, 0.f};
#pragma unroll
    for (int ks = 0; ks < 4; ++ks) {
      const f32x4 a0 = *(LAS f32x4*)(L + L_HB + (128 * w4 + 32 * ks + 8 * q4) * 4), a1 = *(LAS f32x4*)(L + L_HB + (128 * w4 + 32 * ks + 8 * q4 + 4) * 4);
      const v4u hf = {pk2(rbf(a0[0] - mb) * ri, rbf(a0[1] - mb) * ri), pk2(rbf(a0[2] - mb) * ri, rbf(a0[3] - mb) * ri),
                      pk2(rbf(a1[0] - mb) * ri, rbf(a1[1] - mb) * ri), pk2(rbf(a1[2] - mb) * ri, rbf(a1[3] - mb) * ri)};
#pragma unroll
      for (int t = 0; t < 8; ++t) acc[t] = mfma16(hf, gw[8 * ks + t], acc[t]);
    }
    if (q4 == 0) {
      *(LAS f32x4*)(L + L_RED + (w4 * 128 + 8 * g) * 4) = f32x4{acc[0][0], acc[1][0], acc[2][0], acc[3][0]};
      *(LAS f32x4*)(L + L_RED + (w4 * 128 + 8 * g + 4) * 4) = f32x4{acc[4][0], acc[5][0], acc[6][0], acc[7][0]};
    }
  }
  BAR();
  if (w >= 4) {
    const int li = lane < FSW ? lane : 0;
    float gg = 0.f, uu = 0.f;
#pragma unroll
    for (int i = 0; i < 4; ++i) { gg += *(LAS float*)(L + L_RED + (i * 128 + li) * 4); uu += *(LAS float*)(L + L_RED + (i * 128 + FSW + li) * 4); }
    float a = gg / (1.0f + __expf(-gg)) * uu;
    if (lane >= ncol) a = 0.f;
    LAS unsigned char* al = L + L_ACT + w4 * 128;
    *(LAS u16*)(al + lane * 2) = (u16)(pk2(a, a) & 0xFFFFu);
    f32x4 acc[8];
#pragma unroll
    for (int t = 0; t < 8; ++t) acc[t] = f32x4{0.f, 0.f, 0.f, 0.f};
#pragma unroll
    for (int ks = 0; ks < 2; ++ks) {
      const v4u af = *(LAS v4u*)(al + (32 * ks + 8 * q4) * 2);
#pragma unroll
      for (int t = 0; t < 8; ++t) acc[t] = mfma16(af, dw[8 * ks + t], acc[t]);
    }
    if (q4 < 2) {
      v4u o;
#pragma unroll
      for (int e = 0; e < 4; ++e) o[e] = nosent(asu(q4 == 0 ? acc[e][0] : acc[4 + e][0]));
      stc16(rs, OFF_FP + (b * 32 + s) * 2048 + (128 * w4 + 8 * g + 4 * q4) * 4, o);
    }
    TS(6);
  } else if (w == 0 && s < 16) {
    const int c4 = lane & 7, rg = lane >> 3;
    v4u fp[4];
    const unsigned base = OFF_FP + b * 32 * 2048 + (32 * s + 4 * c4) * 4;
    #pragma nounroll
      for (int spin = 0; spin < SPIN_CAP; ++spin) {
        asm volatile("" ::: "memory");
#pragma unroll
      for (int i = 0; i < 4; ++i) { const int r = rg + 8 * i; fp[i] = ldc16(rs, base + (r < NFS ? r : 0) * 2048); }
      const bool ok = umax(umax(mx4(fp[0]), mx4(fp[1])), umax(mx4(fp[2]), mx4(fp[3]))) != SENT;
      if (__all(ok)) break;
      __builtin_amdgcn_s_sleep(1);
    }
    TS(6);
    float v[4];
#pragma unroll
    for (int e = 0; e < 4; ++e) {
      const float last = (rg + 24 < NFS) ? asf(fp[3][e]) : 0.f;
      v[e] = (asf(fp[0][e]) + asf(fp[1][e])) + (asf(fp[2][e]) + last);
    }
#pragma unroll
    for (int e = 0; e < 4; ++e) { v[e] = swap32_add(v[e], v[e]); v[e] = swap16_add(v[e], v[e]); v[e] += ror16<8>(v[e]); }
    const f32x4 hv = *(LAS f32x4*)(L + L_HB + (32 * s + 4 * c4) * 4);
    if (lane < 8) *(GAS v2u*)(out + b * DM + 32 * s + 4 * c4) = v2u{pk2(hv[0] + v[0], hv[1] + v[1]), pk2(hv[2] + v[2], hv[3] + v[3])};
    if (s == 0 && lane == 0) {
      const unsigned e0 = ldc4(rs, OFF_EPOCH + b * 256);
      stc4(rs, OFF_EPOCH + b * 256, e0 + 1u);
    }
    TS(7);
  }
}

extern "C" void kernel_launch(void* const* d_in, const int* in_sizes, int n_in, void* d_out, int out_size, void* d_ws, size_t ws_size,
                              hipStream_t stream) {
  (void)hipFuncSetAttribute((const void*)mega, hipFuncAttributeMaxDynamicSharedMemorySize, LDS_BYTES);
  mega<<<256, 512, LDS_BYTES, stream>>>((const u16*)d_in[0], (const u16*)d_in[1], (const u16*)d_in[2], (const u16*)d_in[3],
                                        (const u16*)d_in[4], (const u16*)d_in[5], (const u16*)d_in[7], (const u16*)d_in[8],
                                        (const u16*)d_in[9], (u16*)d_out, (unsigned char*)d_ws);
}
```

```cpp
#include <hip/hip_runtime.h>
#include <hip/hip_bf16.h>

#define GAS __attribute__((address_space(1)))
#define LAS __attribute__((address_space(3)))
#define DI __device__ __forceinline__
typedef unsigned short u16;
typedef unsigned v4u __attribute__((ext_vector_type(4)));
typedef unsigned v2u __attribute__((ext_vector_type(2)));
typedef float f32x2 __attribute__((ext_vector_type(2)));
typedef float f32x4 __attribute__((ext_vector_type(4)));
typedef float f32x16 __attribute__((ext_vector_type(16)));
typedef short bf16x8 __attribute__((ext_vector_type(8)));
typedef short s16x4 __attribute__((ext_vector_type(4)));
typedef __bf16 bf16x2 __attribute__((ext_vector_type(2)));
typedef __bf16 bfv8 __attribute__((ext_vector_type(8)));
typedef __bf16 bfv16 __attribute__((ext_vector_type(16)));
typedef unsigned v8u __attribute__((ext_vector_type(8)));
typedef __amdgpu_buffer_rsrc_t rsrc_t;

constexpr int NB = 4, NH = 32, SK = 4096, HD = 128, DM = 512, DC = 512, FF = 1376;
constexpr int FSW = 48, NFS = 29;

constexpr unsigned OFF_EPOCH = 0;
constexpr unsigned OFF_SIG3 = 36864;
constexpr unsigned OFF_SIG4 = OFF_SIG3 + 16384;
constexpr unsigned OFF_STAT = OFF_SIG4 + 16384;
constexpr unsigned OFF_QA = OFF_STAT + 65536;
constexpr unsigned OFF_OP = OFF_QA + 262144;
constexpr unsigned OFF_FP = OFF_OP + 131072;
constexpr unsigned OFF_PART = 1u << 20;
constexpr unsigned WS_BYTES = 8u << 20;
static_assert(OFF_FP + 262144 <= OFF_PART, "map");

constexpr int LDS_BYTES = 163840;
constexpr int L_QA = 131072;
constexpr int L_MX = L_QA, L_LS = L_QA + 512, L_P = L_QA + 1024, PSTR = 272;
constexpr int L_HB = L_QA + 16384  , L_RED = L_QA + 18688  , L_ACT = L_QA + 22784;
constexpr int AUX_SC1 = 16;
constexpr unsigned SENT = 0xFFFFFFFFu;
constexpr int SPIN_CAP = 200000;
constexpr unsigned CODE_BYTES = 23 * 1024 + 640;
constexpr int PSLEEP = 2, TDELAY = 20, GMIN = 375, CADELAY = 2, DWDELAY = 22, D2 = 4, D3 = 8, D4 = 4;

DI float asf(unsigned u) { return __builtin_bit_cast(float, u); }
DI unsigned asu(float f) { return __builtin_bit_cast(unsigned, f); }
DI float bflo(unsigned u) { return __builtin_bit_cast(float, u << 16); }
DI float bfhi(unsigned u) { return __builtin_bit_cast(float, u & 0xFFFF0000u); }
DI unsigned pk2(float a, float b) {
  f32x2 v = {a, b};
  return __builtin_bit_cast(unsigned, __builtin_convertvector(v, bf16x2));
}
DI float rbf(float a) { return bflo(pk2(a, a) & 0xFFFFu); }
DI unsigned nosent(unsigned u) { return u < 0xFFFFFFFEu ? u : 0xFFFFFFFEu; }
DI f32x4 mfma16(v4u a, v4u b, f32x4 c) {
  return __builtin_amdgcn_mfma_f32_16x16x32_bf16(__builtin_bit_cast(bf16x8, a), __builtin_bit_cast(bf16x8, b), c, 0, 0, 0);
}
DI f32x16 mfma32(v4u a, v4u b, f32x16 c) {
  return __builtin_amdgcn_mfma_f32_32x32x16_bf16(__builtin_bit_cast(bf16x8, a), __builtin_bit_cast(bf16x8, b), c, 0, 0, 0);
}
template <int N> DI float ror16(float v) {
  return __builtin_bit_cast(float, __builtin_amdgcn_update_dpp(0, __builtin_bit_cast(int, v), 0x120 + N, 0xf, 0xf, false));
}
template <int N> DI float addror(float a, float b) {
  return a + __builtin_bit_cast(float, __builtin_amdgcn_update_dpp(0, __builtin_bit_cast(int, b), 0x120 + N, 0xf, 0xf, true));
}
DI float sum16(float v) { v += ror16<8>(v); v += ror16<4>(v); v += ror16<2>(v); v += ror16<1>(v); return v; }
DI float max16(float v) {
  v = fmaxf(v, ror16<8>(v)); v = fmaxf(v, ror16<4>(v)); v = fmaxf(v, ror16<2>(v)); v = fmaxf(v, ror16<1>(v)); return v;
}
DI float swap32_add(float a, float b) {
  auto r = __builtin_amdgcn_permlane32_swap(__builtin_bit_cast(unsigned, a), __builtin_bit_cast(unsigned, b), false, false);
  return __builtin_bit_cast(float, (unsigned)r[0]) + __builtin_bit_cast(float, (unsigned)r[1]);
}
DI float swap16_add(float a, float b) {
  auto r = __builtin_amdgcn_permlane16_swap(__builtin_bit_cast(unsigned, a), __builtin_bit_cast(unsigned, b), false, false);
  return __builtin_bit_cast(float, (unsigned)r[0]) + __builtin_bit_cast(float, (unsigned)r[1]);
}
DI float swap32_max(float a) {
  auto r = __builtin_amdgcn_permlane32_swap(__builtin_bit_cast(unsigned, a), __builtin_bit_cast(unsigned, a), false, false);
  return fmaxf(__builtin_bit_cast(float, (unsigned)r[0]), __builtin_bit_cast(float, (unsigned)r[1]));
}
DI float swap16_max(float a) {
  auto r = __builtin_amdgcn_permlane16_swap(__builtin_bit_cast(unsigned, a), __builtin_bit_cast(unsigned, a), false, false);
  return fmaxf(__builtin_bit_cast(float, (unsigned)r[0]), __builtin_bit_cast(float, (unsigned)r[1]));
}
DI float dot2(unsigned a, unsigned b, float c) {
  return __builtin_amdgcn_fdot2_f32_bf16(__builtin_bit_cast(bf16x2, a), __builtin_bit_cast(bf16x2, b), c, false);
}
DI float sum64(float v) { v = sum16(v); v = swap16_add(v, v); return swap32_add(v, v); }
DI unsigned plo(unsigned a, unsigned b) { return __builtin_amdgcn_perm(b, a, 0x05040100u); }
DI unsigned phi(unsigned a, unsigned b) { return __builtin_amdgcn_perm(b, a, 0x07060302u); }
DI void tr16(const v4u* R, v8u* F) {
#pragma unroll
  for (int p = 0; p < 8; ++p)
#pragma unroll
    for (int t = 0; t < 8; ++t) F[t][p] = (t & 1) ? phi(R[2 * p][t >> 1], R[2 * p + 1][t >> 1]) : plo(R[2 * p][t >> 1], R[2 * p + 1][t >> 1]);
}
DI f32x4 smf(v4u a, v8u b, f32x4 c, unsigned idx) {
  return __builtin_amdgcn_smfmac_f32_16x16x64_bf16(__builtin_bit_cast(bfv8, a), __builtin_bit_cast(bfv16, b), c, (int)idx, 0, 0);
}
#define ZACC(acc) _Pragma("unroll") for (int t_ = 0; t_ < 8; ++t_) { acc[t_] = f32x4{0.f, 0.f, 0.f, 0.f}; asm volatile("" : "+v"(acc[t_])); }
#define BAR() do { asm volatile("s_waitcnt lgkmcnt(0)" ::: "memory"); __builtin_amdgcn_s_barrier(); asm volatile("" ::: "memory"); } while (0)
#define RAWBAR() do { asm volatile("" ::: "memory"); __builtin_amdgcn_s_barrier(); asm volatile("" ::: "memory"); } while (0)
#define DRAIN() asm volatile("s_waitcnt vmcnt(0)" ::: "memory")
#define SCHED() __builtin_amdgcn_sched_barrier(0)
DI rsrc_t mk(const void* p, unsigned bytes) { return __builtin_amdgcn_make_buffer_rsrc((void*)p, 0, (int)bytes, 0x00020000); }
DI v4u ldw16(rsrc_t r, unsigned voff, unsigned soff) { return __builtin_amdgcn_raw_buffer_load_b128(r, voff, soff, 0); }
DI v4u ldc16(rsrc_t rs, unsigned off) { return __builtin_amdgcn_raw_buffer_load_b128(rs, off, 0, AUX_SC1); }
DI v2u ldc8(rsrc_t rs, unsigned off) { return __builtin_amdgcn_raw_buffer_load_b64(rs, off, 0, AUX_SC1); }
DI unsigned ldc4(rsrc_t rs, unsigned off) { return __builtin_amdgcn_raw_buffer_load_b32(rs, off, 0, AUX_SC1); }
DI void stc16(rsrc_t rs, unsigned off, v4u v) { __builtin_amdgcn_raw_buffer_store_b128(v, rs, off, 0, AUX_SC1); }
DI void stc8(rsrc_t rs, unsigned off, v2u v) { __builtin_amdgcn_raw_buffer_store_b64(v, rs, off, 0, AUX_SC1); }
DI void stc4(rsrc_t rs, unsigned off, unsigned v) { __builtin_amdgcn_raw_buffer_store_b32(v, rs, off, 0, AUX_SC1); }
DI unsigned umax(unsigned a, unsigned b) { return a > b ? a : b; }
DI unsigned mx4(v4u v) { return umax(umax(v.x, v.y), umax(v.z, v.w)); }

#define SPIN _Pragma("nounroll") for (int spin = 0; spin < SPIN_CAP; ++spin)
#define PIN(x) asm volatile("" : "+v"(x))
#define FRESH() asm volatile("" ::: "memory")
DI void touch(const void* base, unsigned bytes, unsigned voff) {
  const unsigned long long a = (unsigned long long)base;
  const v4u srd = {(unsigned)a, (unsigned)(a >> 32) & 0xFFFFu, bytes, 0x00020000u};
  asm volatile("buffer_load_dword v255, %0, %1, 0 offen" ::"v"(voff), "s"(srd) : "v255");
}

#define GATHER_COMBINE(gbase, AOP, INIT, dst, SHADOW, AFTER)                                             \
  {                                                                                                      \
    f32x4 v_;                                                                                            \
    SPIN {                                                                                               \
      FRESH();                                                                                           \
      v4u p_[4];                                                                                         \
      _Pragma("unroll") for (int i = 0; i < 4; ++i) p_[i] = __builtin_amdgcn_raw_buffer_load_b128(rs, gbase, i * 8192, AUX_SC1); \
      v_ = INIT;                                                                                         \
      SHADOW;                                                                                            \
      unsigned m_ = 0;                                                                                   \
      SCHED();                                                                                           \
      _Pragma("unroll") for (int i = 0; i < 4; ++i) {                                                    \
        v_ = mfma16(AOP, p_[i], v_);                                                                     \
        m_ = umax(umax(m_, p_[i].x), p_[i].y);                                                           \
        m_ = umax(umax(m_, p_[i].z), p_[i].w);                                                           \
        SCHED();                                                                                         \
      }                                                                                                  \
      PIN(v_);                                                                                           \
      if (__all(m_ != SENT)) break;                                                                      \
    }                                                                                                    \
    AFTER;                                                                                               \
    float u_[4];                                                                                         \
    _Pragma("unroll") for (int e = 0; e < 4; ++e) {                                                      \
      const float t_ = v_[e];                                                                            \
      u_[e] = asf(__builtin_amdgcn_update_dpp(asu(t_), asu(t_), 0x128, 0xc, 0xf, false));                \
    }                                                                                                    \
    float s0_ = swap32_add(u_[0], u_[2]), s1_ = swap32_add(u_[1], u_[3]);                                \
    PIN(s0_); PIN(s1_);                                                                                  \
    *(LAS unsigned*)(size_t)(dst) = pk2(s0_, s1_);                                                       \
  }
#define QUADSUM(o, acc) _Pragma("unroll") for (int e_ = 0; e_ < 4; ++e_) { float t_ = swap16_add(acc[e_][0], acc[4 + e_][0]); PIN(t_); o[e_] = t_; }

__global__ void __launch_bounds__(512, 2) __attribute__((amdgpu_num_vgpr(255)))
mega(const u16* __restrict__ x, const u16* __restrict__ ckv, const u16* __restrict__ wq, const u16* __restrict__ wuk,
     const u16* __restrict__ wuv, const u16* __restrict__ wo, const u16* __restrict__ wg, const u16* __restrict__ wu,
     const u16* __restrict__ wd, u16* __restrict__ out, unsigned char* ws) {
  extern __shared__ __attribute__((aligned(16))) unsigned char smem[];
  LAS unsigned char* L = (LAS unsigned char*)smem;
  if ((unsigned)(size_t)L != 0u) return;
  const int tid = threadIdx.x, lane = tid & 63, w = __builtin_amdgcn_readfirstlane(tid >> 6);
  const int bid = blockIdx.x, xcd = bid & 7, idx = bid >> 3;
  const int q4 = lane >> 4, g = lane & 15, ck = lane & 7, rg = lane >> 3;
  const unsigned sidx = (lane & 1) ? 0xEEEEu : 0x4444u, sidq = (lane & 4) ? 0xEEEEu : 0x4444u;
  const v4u sent4 = {SENT, SENT, SENT, SENT};
  const int pq = 2 * (q4 & 1) + (q4 >> 1);
#define ONEHOT(oh) v4u oh; _Pragma("unroll") for (int e = 0; e < 4; ++e) oh[e] = (ck >> 1) == e ? ((ck & 1) ? 0xFFFF0000u : 0x0000FFFFu) : 0u; PIN(oh)
  if (w == 7 && lane < 16) touch((const void*)__builtin_amdgcn_s_getpc(), CODE_BYTES, (idx * 16 + lane) * 128);
#define ARGS() asm volatile("" ::"s"(x), "s"(ckv), "s"(wq), "s"(wuk), "s"(wuv), "s"(wo), "s"(wg), "s"(wu), "s"(wd), "s"(out), "s"(ws))
#define rs mk(ws, WS_BYTES)

  if (__builtin_expect(idx < 16, 1)) {
    const int h = xcd * 4 + (idx >> 2), j = idx & 3;
    const int k0 = 32 * (j & 1), k1 = k0 ^ 32;
    const unsigned wvo = q4 * 8 * 8192 + g * 16;
    v4u tags = {0u, 0u, 0u, 0u};
    if (__builtin_expect(w < 4, 1)) {
      ARGS();
      const rsrc_t rq = mk(wq, DM * NH * HD * 2);
      v4u wq4[32];
#pragma unroll
      for (int i = 0; i < 32; ++i) wq4[i] = ldw16(rq, wvo, (128 * (((i >> 3) + j) & 3) + 32 * w + (i & 7)) * 8192 + h * 256);
      if (w == 0) tags = ldc16(rs, OFF_EPOCH);
      SCHED();
      unsigned xr0 = 24576 + (g >> 2) * 1024 + (g & 1) * 512 + 128 * (((q4 >> 1) + j) & 3) + 32 * w + 16 * (q4 & 1);
      unsigned qw = (w * 512 + 8 * lane) * 4;
      PIN(xr0); PIN(qw);
      f32x4 acc[8];
      ZACC(acc);
      RAWBAR();
      const v4u xf0 = *(LAS v4u*)(size_t)(xr0), xf1 = *(LAS v4u*)(size_t)(xr0 ^ 256);
      v8u wf[8];
      tr16(wq4, wf);
#pragma unroll
      for (int t = 0; t < 8; ++t) acc[t] = smf(xf0, wf[t], acc[t], sidx);
      SCHED();
      tr16(wq4 + 16, wf);
#pragma unroll
      for (int t = 0; t < 8; ++t) acc[t] = smf(xf1, wf[t], acc[t], sidx);
      SCHED();
      float r[8];
#pragma unroll
      for (int t = 0; t < 8; ++t) { r[t] = acc[t][0] + acc[t][1]; PIN(r[t]); }
      *(LAS f32x4*)(size_t)(qw) = f32x4{r[0], r[1], r[2], r[3]};
      *(LAS f32x4*)(size_t)(qw + 16) = f32x4{r[4], r[5], r[6], r[7]};
      BAR();
    } else {
      ARGS();
      const int wk4 = w - 4;
      __builtin_amdgcn_s_setprio(3);
      const v4u xs = *(const GAS v4u*)(x + wk4 * DM + lane * 8);
      tags = ldc16(rs, OFF_EPOCH);
      {
        float v[8];
#pragma unroll
        for (int e = 0; e < 4; ++e) { v[2 * e] = bflo(xs[e]); v[2 * e + 1] = bfhi(xs[e]); }
        const float mean = sum64(((v[0] + v[1]) + (v[2] + v[3])) + ((v[4] + v[5]) + (v[6] + v[7]))) * (1.0f / DM);
        float s2 = 0.f;
#pragma unroll
        for (int e = 0; e < 8; ++e) s2 += (v[e] - mean) * (v[e] - mean);
        const float var = rbf(sum64(s2) * (1.0f / DM));
        const float ri = __builtin_amdgcn_rcpf(rbf(__builtin_amdgcn_sqrtf(rbf(var + 1e-5f)))), mb = rbf(mean);
        v4u o;
#pragma unroll
        for (int e = 0; e < 4; ++e) o[e] = pk2(rbf(v[2 * e] - mb) * ri, rbf(v[2 * e + 1] - mb) * ri);
        *(LAS v2u*)(L + 24576 + wk4 * 1024 + lane * 8) = v2u{o[0], o[2]};
        *(LAS v2u*)(L + 24576 + wk4 * 1024 + 512 + lane * 8) = v2u{o[1], o[3]};
      }
      BAR();
      const rsrc_t rk = mk(wuk, DC * NH * HD * 2);
      v4u wk[8];
#pragma unroll
      for (int i = 0; i < 8; ++i) wk[i] = ldw16(rk, g * 8192 + q4 * 16 + (i & 3) * 64, (128 * j + 32 * wk4 + 16 * (i >> 2)) * 8192 + h * 256);
      unsigned tg = ((lane & 2) ? ((lane & 1) ? tags.w : tags.z) : ((lane & 1) ? tags.y : tags.x)) + 1u;
      unsigned qo = g < 8 ? OFF_QA + ((g & 3) * NH + h) * 2048 + 512 * j + 128 * wk4 + 16 * (g & 4) + 16 * q4 : 0xFFFFFF00u;
      unsigned pr = lane * 32, qw = 16384 + wk4 * 1024 + lane * 16, qr = 16384 + wk4 * 1024 + (lane & 3) * 256 + q4 * 16;
      PIN(tg); PIN(qo); PIN(pr); PIN(qw); PIN(qr);
      RAWBAR();
      f32x4 pp[8];
#pragma unroll
      for (int i = 0; i < 8; ++i) pp[i] = *(LAS f32x4*)(size_t)(pr + (i >> 1) * 2048 + (i & 1) * 16);
      const f32x4 s0 = (pp[0] + pp[2]) + (pp[4] + pp[6]), s1 = (pp[1] + pp[3]) + (pp[5] + pp[7]);
      *(LAS v4u*)(size_t)(qw) = v4u{pk2(s0[0], s0[1]), pk2(s0[2], s0[3]), pk2(s1[0], s1[1]), pk2(s1[2], s1[3])};
      v4u qf[4];
#pragma unroll
      for (int ks = 0; ks < 4; ++ks) qf[ks] = *(LAS v4u*)(size_t)(qr + 64 * ks);
      SCHED();
      f32x4 a0 = {0.f, 0.f, 0.f, 0.f}, a1 = {0.f, 0.f, 0.f, 0.f};
#pragma unroll
      for (int ks = 0; ks < 4; ++ks) { a0 = mfma16(wk[ks], qf[ks], a0); a1 = mfma16(wk[4 + ks], qf[ks], a1); }
      const float sc = 0.08838834764831845f * 1.4426950408889634f;
      const f32x4 av = (g & 4) ? a1 : a0;
      stc16(rs, qo, v4u{pk2(av[0] * sc, av[1] * sc), tg, pk2(av[2] * sc, av[3] * sc), tg});
      __builtin_amdgcn_s_setprio(0);
    }
    BAR();
    if (w == 0) stc16(rs, OFF_OP + (j * NH + h) * 1024 + lane * 16, sent4);
    v4u wr[16], wo8[16];
    const rsrc_t rv = mk(wuv, DC * NH * HD * 2), ro = mk(wo, NH * HD * DM * 2);
    const int d0 = 64 * (j & 1), d1 = d0 ^ 64;
    const unsigned tagj = (j == 0 ? tags.x : (j == 1 ? tags.y : (j == 2 ? tags.z : tags.w))) + 1u;
    unsigned sbase = OFF_STAT + ((j * 32 + (lane & 31)) * NH + h) * 8;
    unsigned pbase = OFF_PART + (j * NH + h) * 32768 + rg * 1024 + w * 128 + ck * 16;
    unsigned c1w = 20480 + (g >> 3) * 1024 + w * 128 + (pq & 1) * 64 + (2 * ck + (pq >> 1)) * 4;
    unsigned c1r = 20480 + w * 128 + (g & 4) * 16 + ((q4 < 2 ? k0 : k1) & 32) + 16 * (q4 & 1);
    unsigned r1w = (w * 128 + 8 * g + 4 * (q4 & 1)) * 4 + (q4 >> 1) * 4096;
    unsigned r1r = lane * 8;
    unsigned c2w = 8192 + w * 256 + (lane & 1) * 128 + (lane >> 1) * 4;
    unsigned c2r = 8192 + w * 256 + (g & 4) * 32 + (q4 < 2 ? d0 : d1) + 32 * (g & 1) + 16 * (q4 & 1);
    unsigned opo = (lane & 40) == 0 ? OFF_OP + (j * NH + h) * 1024 + 128 * w + 16 * ck + 8 * q4 : 0xFFFFFF00u;
    unsigned s3o = lane == 0 ? OFF_SIG3 + (j * NH + h) * 128 : 0xFFFFFF00u;
    PIN(sbase); PIN(pbase); PIN(c1w); PIN(c1r); PIN(r1w); PIN(r1r); PIN(c2w); PIN(c2r); PIN(opo); PIN(s3o);
#define LD_UV(lo, hi) _Pragma("unroll") for (int i = lo; i < hi; ++i) wr[i] = ldw16(rv, wvo, (w * 64 + ((i < 8 ? k0 : k1) & 32) + (i & 7)) * 8192 + h * 256)
#define LD_O(lo, hi)                               \
  _Pragma("unroll") for (int i = lo; i < hi; ++i)  \
      wo8[i] = ldw16(ro, ((g >> 3) * 32 + q4 * 8) * 1024 + ck * 16 + (i & 3) * 1024, (h * HD + (i < 8 ? d0 : d1) + (i & 4)) * 1024 + 128 * w)
    v8u uf[8], of[8];
#define PIN8(F) _Pragma("unroll") for (int i = 0; i < 8; ++i) PIN(F[i])
#define ISSUED() do { SCHED(); if (w >= 4) RAWBAR(); } while (0)
    if (j & 2) {
      LD_O(0, 8); SCHED(); RAWBAR(); LD_O(8, 16); SCHED(); LD_UV(0, 16); ISSUED();
      tr16(wo8, of); PIN8(of); SCHED(); tr16(wr, uf); PIN8(uf);
      asm volatile("; o" ::: "memory");
    } else {
      LD_UV(0, 8); SCHED(); RAWBAR(); LD_UV(8, 16); SCHED(); LD_O(0, 16); ISSUED();
      tr16(wr, uf); PIN8(uf); SCHED(); tr16(wo8, of); PIN8(of);
      asm volatile("; u" ::: "memory");
    }
    f32x4 acc1[8], acc2[8];
    ONEHOT(oh);
    if (w < 4) BAR();
    float ew = 0.f;
    v2u sl = {0u, 0u};
    if (w == 0) {
      DRAIN();
      v2u st;
      SPIN {
        FRESH();
        st = ldc8(rs, sbase);
        if (__all(st.y == tagj)) break;
        __builtin_amdgcn_s_sleep(PSLEEP);
      }
      const float m = asf(st.x);
      const float e = __builtin_amdgcn_exp2f(m - swap16_max(max16(m)));
      const unsigned eb2 = pk2(e, e);
      ew = bfhi(eb2);
      *(LAS unsigned*)(L + 28672 + ((lane & 7) * 4 + ((lane >> 3) & 3)) * 4) = eb2;
      __builtin_amdgcn_s_sleep(D2);
    }
    BAR();
    if (w == 0) sl = ldc8(rs, sbase + 32768);
    const v4u wl = *(LAS v4u*)(L + 28672 + rg * 16);
    v4u aw4[4];
    GATHER_COMBINE(pbase, aw4[i], (f32x4{0.f, 0.f, 0.f, 0.f}), c1w,
                   _Pragma("unroll") for (int i = 0; i < 4; ++i) { aw4[i] = oh & wl[i]; PIN(aw4[i]); } ZACC(acc1),
                   if (w == 7) stc4(rs, s3o, tagj));
    {
      const v4u a0 = *(LAS v4u*)(size_t)(c1r);
      SCHED();
      ZACC(acc2);
      if (w == 0) {
        if (__builtin_expect(!__all(sl.y == tagj), 0)) {
          SPIN {
            FRESH();
            sl = ldc8(rs, sbase + 32768);
            if (__all(sl.y == tagj)) break;
          }
        }
        float tot = sum16(ew * asf(sl.x));
        tot = swap16_add(tot, tot);
        *(LAS float*)(L + 28928) = __builtin_amdgcn_rcpf(tot);
      }
#pragma unroll
      for (int t = 0; t < 8; ++t) acc1[t] = smf(a0, uf[t], acc1[t], sidq);
      SCHED();
      f32x4 o;
      QUADSUM(o, acc1);
      *(LAS f32x4*)(size_t)(r1w) = o;
    }
    BAR();
    {
      f32x2 sv[8];
#pragma unroll
      for (int i = 0; i < 8; ++i) sv[i] = *(LAS f32x2*)(size_t)(r1r + i * 512);
      const float il = *(LAS float*)(L + 28928);
      const f32x2 s = (((sv[0] + sv[1]) + (sv[2] + sv[3])) + ((sv[4] + sv[5]) + (sv[6] + sv[7]))) * il;
      *(LAS unsigned*)(size_t)(c2w) = pk2(s[0], s[1]);
      const v4u a0 = *(LAS v4u*)(size_t)(c2r);
#pragma unroll
      for (int t = 0; t < 8; ++t) acc2[t] = smf(a0, of[t], acc2[t], sidq);
      SCHED();
      float u[8];
#pragma unroll
      for (int t = 0; t < 8; ++t) { u[t] = addror<8>(acc2[t][0], acc2[t][1]); PIN(u[t]); }
#pragma unroll
      for (int t = 0; t < 4; ++t) { u[t] = swap16_add(u[t], u[t + 4]); PIN(u[t]); }
      stc8(rs, opo, v2u{nosent(pk2(u[0], u[1])), nosent(pk2(u[2], u[3]))});
    }
    return;
  }

  const int b = idx & 3, s = xcd * 4 + ((idx - 16) >> 2);
  const int ba = xcd >> 1, sa_ = 16 * (xcd & 1) + (idx - 16);
  const bool ffn = s < NFS;
  const int r32 = lane & 31, hf = lane >> 5;
  const int w4 = w - 4;
  const int ncol = (s == NFS - 1) ? (FF - FSW * (NFS - 1)) : FSW;
  unsigned tag = ldc4(rs, OFF_EPOCH + b * 4), taga = ldc4(rs, OFF_EPOCH + ba * 4);
  const int cg = g < 6 ? g : (g < 12 ? g - 6 : 0);
  const GAS unsigned char* wsel =
      (const GAS unsigned char*)((g >= 6 && g < 12) ? wu : wg) + (q4 * 8 * FF + FSW * (ffn ? s : 0) + ((8 * cg < ncol) ? 8 * cg : 0)) * 2;
#define GWLD(i) gw[i] = *(const GAS v4u*)(wsel + (size_t)(128 * w4 + 32 * ((((i) >> 3) + b) & 3) + ((i) & 7)) * (FF * 2))
  v4u ca[16];
  unsigned cad[4];
  {
    const int blk = (lane >> 4) & 1, qq = (lane & 15) >> 2, pp = lane & 3;
#pragma unroll
    for (int i = 0; i < 4; ++i) {
      const int ct = i >> 1, t = i & 1, f = (qq << 2) | ((2 * hf + t) & 3), ch = 8 * w + 4 * ct + 2 * blk + (pp >> 1);
      cad[i] = (ch >> 4) * 32768 + (8 * hf + 4 * t + qq) * 256 + 16 * ((ch & 15) ^ f) + 8 * (pp & 1);
    }
  }
#define CA1(n)                                                                                                                    \
  {                                                                                                                               \
    const s16x4 t0_ = __builtin_amdgcn_ds_read_tr16_b64_v4i16((LAS s16x4*)(size_t)(cad[2 * ((n) >> 3)] + ((n) & 7) * 4096));         \
    const s16x4 t1_ = __builtin_amdgcn_ds_read_tr16_b64_v4i16((LAS s16x4*)(size_t)(cad[2 * ((n) >> 3) + 1] + ((n) & 7) * 4096));     \
    ca[n] = __builtin_bit_cast(v4u, __builtin_shufflevector(t0_, t1_, 0, 1, 2, 3, 4, 5, 6, 7));                                   \
  }
#define LOAD_CA() _Pragma("unroll") for (int n_ = 0; n_ < 16; ++n_) CA1(n_)
  auto pv_publish = [&](unsigned pr, unsigned sw, unsigned sr, unsigned po) {
    v4u pfr[8];
#pragma unroll
    for (int ks = 0; ks < 8; ++ks) pfr[ks] = *(LAS v4u*)(size_t)(pr + 32 * ks);
    if (w == 4 && lane < 32) {
      const f32x4 l4 = *(LAS f32x4*)(L + L_LS + r32 * 16);
      stc8(rs, OFF_STAT + 32768 + ((ba * 32 + sa_) * NH + lane) * 8, v2u{asu((l4[0] + l4[1]) + (l4[2] + l4[3])), taga});
    }
    SCHED();
    f32x16 o0, o1;
#pragma unroll
    for (int i = 0; i < 16; ++i) { o0[i] = 0.f; o1[i] = 0.f; }
#pragma unroll
    for (int ks = 0; ks < 8; ++ks) o0 = mfma32(ca[ks], pfr[ks], o0);
    SCHED();
#pragma unroll
    for (int ks = 0; ks < 8; ++ks) {
      o1 = mfma32(ca[8 + ks], pfr[ks], o1);
      if (ks >= 2 && ks < 6)
        *(LAS v2u*)(size_t)((sw ^ (16 * (ks - 2)))) = v2u{pk2(o0[4 * ks - 8], o0[4 * ks - 7]), pk2(o0[4 * ks - 6], o0[4 * ks - 5])};
      SCHED();
    }
#pragma unroll
    for (int gq = 0; gq < 4; ++gq) *(LAS v2u*)(size_t)((sw ^ (64 + 16 * gq))) = v2u{pk2(o1[4 * gq], o1[4 * gq + 1]), pk2(o1[4 * gq + 2], o1[4 * gq + 3])};
    v4u ov[4];
#pragma unroll
    for (int i = 0; i < 4; ++i) ov[i] = *(LAS v4u*)(size_t)(sr + i * 1024);
#pragma unroll
    for (int i = 0; i < 4; ++i) __builtin_amdgcn_raw_buffer_store_b128(ov[i], rs, po, i * 262144, AUX_SC1);
  };
#define FTG(v) touch((v) < 2 ? wg : wu, DM * FF * 2, (32 * (idx - 16) + 16 * ((v) & 1) + (lane >> 2)) * (FF * 2) + xcd * 384 + ((lane & 3) < 3 ? (lane & 3) * 128 : 380))
#define FTD(v) touch(wd, FF * DM * 2, lane < 24 ? (xcd * 1536 + (idx - 16) * 96 + (v) * 24 + lane) * 128 : 0x80000000u)
#define PV_ADDR()                                                                        \
  unsigned pr = L_P + r32 * PSTR + 16 * hf;                                              \
  unsigned sw = w * 4096 + r32 * 128 + 16 * (r32 & 7) + 8 * hf;                          \
  unsigned sr = w * 4096 + rg * 128 + 16 * (ck ^ rg);                                    \
  unsigned po = OFF_PART + ((ba * NH + rg) * 32 + sa_) * 1024 + 128 * w + 16 * ck;        \
  PIN(pr); PIN(sw); PIN(sr); PIN(po)
#define H3_PREP()                                                                        \
  f32x4 xi = {bflo(xv.x), bfhi(xv.x), bflo(xv.y), bfhi(xv.y)};                           \
  if (lane >= 32) xi = f32x4{0.f, 0.f, 0.f, 0.f};                                        \
  unsigned obase = OFF_OP + (b * NH + rg) * 1024 + 128 * w + 16 * ck;                    \
  unsigned hbw = L_HB + (g >> 3) * 8192 + (pq & 1) * 512 + (16 * w + 2 * ck + (pq >> 1)) * 4;   \
  ONEHOT(oh);                                                                            \
  oh &= 0x3F803F80u;                                                                     \
  PIN(obase); PIN(hbw); PIN(xi); PIN(oh)
    const rsrc_t rc = mk(ckv, NB * SK * DC * 2);
    const unsigned cbase = (ba * SK + sa_ * 128 + 32 * (w & 3)) * 1024;
    unsigned cvo[4];
#pragma unroll
    for (int i = 0; i < 4; ++i) cvo[i] = q4 * 1024 + (g ^ ((q4 << 2) | i)) * 16;
  if (__builtin_expect(w < 4, 1)) {
    ARGS();
    __builtin_amdgcn_s_sleep(TDELAY);
    touch(wuk, DC * NH * HD * 2, (32 * (idx - 16) + 8 * w + rg) * 8192 + xcd * 1024 + ck * 128);
#pragma nounroll
    for (int i = 0; i < 8; ++i)
#pragma unroll
      for (int blk = 0; blk < 3; ++blk)
        __builtin_amdgcn_raw_ptr_buffer_load_lds(rc, (LAS void*)(L + blk * 32768 + (32 * w + 4 * i) * 256), 16,
                                                 q4 * 1024 + (g ^ ((q4 << 2) | (i & 3))) * 16, cbase + i * 4096 + blk * 256, 0,   2);
    const unsigned clo = OFF_PART + ((ba * NH + 8 * w) * 32 + sa_) * 1024 + lane * 16;
#define CLR(i) __builtin_amdgcn_raw_buffer_store_b128(sent4, rs, clo, (i) * 32768, AUX_SC1)
    __builtin_amdgcn_s_waitcnt(0x0F70);
    DRAIN();
    asm volatile("" : "+v"(tag));
    tag += 1u;
    v4u ka[32], qb[32];
    const int fr = ((r32 & 3) << 2) | ((r32 >> 2) & 3);
#define KA(ks) (*(LAS v4u*)(L + ((ks) >> 3) * 32768 + (32 * w + r32) * 256 + 16 * (((2 * (ks) + hf) & 15) ^ fr)))
#define QB(ks) (*(LAS v4u*)(L + L_QA + ((ks) >> 4) * 16384 + r32 * 512 + 16 * (((2 * (ks) + hf) & 15) ^ fr) + 256 * (((ks) >> 3) & 1)))
#pragma unroll
    for (int i = 0; i < 8; ++i) CLR(i);
    SCHED();
#pragma unroll
    for (int ks = 0; ks < 24; ++ks) ka[ks] = KA(ks);
    SCHED();
    PV_ADDR();
    unsigned mxw = L_MX + (r32 * 4 + w) * 4, mxr = L_MX + r32 * 16, pw = L_P + r32 * PSTR + (32 * w + 4 * hf) * 2;
    PIN(mxw); PIN(mxr); PIN(pw);
    RAWBAR();
    {
      const unsigned long long ca_ = (unsigned long long)ckv;
      const v4u srd_ = {(unsigned)ca_, (unsigned)(ca_ >> 32) & 0xFFFFu, (unsigned)(NB * SK * DC * 2), 0x00020000u};
#pragma unroll
      for (int i = 0; i < 8; ++i) {
        unsigned keep_;
        asm volatile("s_mov_b32 %0, m0\n\ts_mov_b32 m0, %3\n\ts_nop 0\n\tbuffer_load_dwordx4 %1, %2, %4 offen nt lds\n\ts_mov_b32 m0, %0"
                     : "=&s"(keep_) : "v"(cvo[i & 3]), "s"(srd_), "s"(3 * 32768 + (32 * w + 4 * i) * 256), "s"(cbase + i * 4096 + 768) : "memory");
      }
    }
    RAWBAR();
    f32x16 sa;
#pragma unroll
    for (int i = 0; i < 16; ++i) sa[i] = 0.f;
#pragma unroll
    for (int ks = 0; ks < 8; ++ks) { qb[ks] = QB(ks); SCHED(); }
#pragma unroll
    for (int ks = 0; ks < 8; ++ks) { sa = mfma32(ka[ks], qb[ks], sa); qb[8 + ks] = QB(8 + ks); SCHED(); }
#pragma unroll
    for (int ks = 8; ks < 12; ++ks) sa = mfma32(ka[ks], qb[ks], sa);
    SCHED();
    BAR();
#pragma unroll
    for (int ks = 12; ks < 16; ++ks) {
      qb[2 * ks - 8] = QB(2 * ks - 8);
      qb[2 * ks - 7] = QB(2 * ks - 7);
      sa = mfma32(ka[ks], qb[ks], sa);
      SCHED();
    }
#pragma unroll
    for (int ks = 16; ks < 24; ++ks) {
      sa = mfma32(ka[ks], qb[ks], sa);
      if (ks < 20) { qb[2 * ks - 8] = QB(2 * ks - 8); qb[2 * ks - 7] = QB(2 * ks - 7); }
      CA1(2 * (ks - 16)); CA1(2 * (ks - 16) + 1);
      SCHED();
    }
    DRAIN();
#pragma unroll
    for (int ks = 24; ks < 32; ++ks) { ka[ks] = KA(ks); SCHED(); }
#pragma unroll
    for (int ks = 24; ks < 32; ++ks) { sa = mfma32(ka[ks], qb[ks], sa); SCHED(); }
    float mx = fmaxf(fmaxf(fmaxf(fmaxf(sa[0], sa[1]), sa[2]), fmaxf(fmaxf(sa[3], sa[4]), sa[5])),
                     fmaxf(fmaxf(fmaxf(sa[6], sa[7]), sa[8]), fmaxf(fmaxf(sa[9], sa[10]), sa[11])));
    mx = fmaxf(mx, fmaxf(fmaxf(fmaxf(sa[12], sa[13]), sa[14]), sa[15]));
    mx = swap32_max(mx);
    *(LAS float*)(size_t)(mxw) = mx;
    BAR();
    {
      const f32x4 m4 = *(LAS f32x4*)(size_t)(mxr);
      mx = fmaxf(fmaxf(m4[0], m4[1]), fmaxf(m4[2], m4[3]));
    }
    float ls = 0.f;
#pragma unroll
    for (int gq = 0; gq < 4; ++gq) {
      float pv[4];
#pragma unroll
      for (int e = 0; e < 4; ++e) pv[e] = __builtin_amdgcn_exp2f(sa[4 * gq + e] - mx);
      ls += (pv[0] + pv[1]) + (pv[2] + pv[3]);
      *(LAS v2u*)(size_t)(pw + 16 * gq) = v2u{pk2(pv[0], pv[1]), pk2(pv[2], pv[3])};
    }
    ls = swap32_add(ls, ls);
    *(LAS float*)(size_t)(mxw + 512) = ls;
    BAR();
    pv_publish(pr, sw, sr, po);
    if (!ffn) return;
    BAR();
    const v2u xv = *(const GAS v2u*)(x + b * DM + 64 * w + 8 * ck + 4 * (q4 & 1));
    SCHED();
    FTD(w);
    v8u df[8];
    {
      __builtin_amdgcn_s_sleep(DWDELAY);
      v4u dw[16];
      const rsrc_t rd = mk(wd, FF * DM * 2);
#pragma unroll
    for (int i = 0; i < 16; ++i) {
      const int k = 32 * (i >> 3) + 8 * q4;
      dw[i] = ldw16(rd, (k < ncol ? k * 1024 + g * 16 : 0x80000000u) + (i & 3) * 1024, (FSW * s + (i & 4)) * 1024 + 256 * w);
    }
      SCHED();
      tr16(dw, df);
#pragma unroll
      for (int i = 0; i < 8; ++i) asm volatile("" : "+v"(df[i]));
    }
    H3_PREP();
    f32x4 acc[8];
    unsigned s4o = tid == 0 ? OFF_SIG4 + (b * 32 + s) * 128 : 0xFFFFFF00u;
    unsigned rdr = L_RED + (lane < FSW ? lane : 0) * 4;
    unsigned aw = L_ACT + w * 128 + ((lane >> 1) & 1) * 64 + (lane >> 2) * 4 + (lane & 1) * 2;
    unsigned ar = L_ACT + w * 128 + (g & 4) * 16 + q4 * 16;
    unsigned fo = q4 < 2 ? OFF_FP + (b * 32 + s) * 2048 + (128 * w + 8 * g + 4 * q4) * 4 : 0xFFFFFF00u;
    float amask = lane < ncol ? 1.0f : 0.0f;
    unsigned hbr = L_HB + lane * 16, one2 = 0x3F803F80u;
    PIN(s4o); PIN(rdr); PIN(aw); PIN(ar); PIN(fo); PIN(amask); PIN(hbr);
    asm volatile("" : "+s"(one2));
    if (w == 1) {
      *(LAS v4u*)(L + L_HB + 1024 + lane * 16) = v4u{0x3F803F80u, 0x3F803F80u, 0x3F803F80u, 0x3F803F80u};
    } else if (w == 0) {
      SPIN {
        FRESH();
        const unsigned t = ldc4(rs, OFF_SIG3 + (b * NH + r32) * 128);
        if (__all(t == tag)) break;
        __builtin_amdgcn_s_sleep(PSLEEP);
      }
      __builtin_amdgcn_s_sleep(D3);
    }
    BAR();
    GATHER_COMBINE(obase, oh, xi, hbw, ZACC(acc), if (w == 0) stc4(rs, s4o, tag));
    BAR();
    float mb, ri;
    {
      const v4u hv = *(LAS v4u*)(size_t)(hbr);
      float s1 = 0.f, s2 = 0.f;
#pragma unroll
      for (int e = 0; e < 4; ++e) { s1 = dot2(hv[e], one2, s1); s2 = dot2(hv[e], hv[e], s2); }
      float t = swap32_add(s1, s2);
      t = sum16(swap16_add(t, t));
      const float mean = asf(__builtin_amdgcn_readlane(asu(t), 0)) * (1.0f / DM);
      const float var = rbf(fmaxf(asf(__builtin_amdgcn_readlane(asu(t), 32)) * (1.0f / DM) - mean * mean, 0.f));
      ri = __builtin_amdgcn_rcpf(rbf(__builtin_amdgcn_sqrtf(rbf(var + 1e-5f))));
      mb = rbf(mean);
      PIN(ri); PIN(mb);
    }
    BAR();
    {
      float pg[4], pu[4], cgs[4], cus[4];
#pragma unroll
      for (int i = 0; i < 4; ++i) {
        pg[i] = *(LAS float*)(size_t)(rdr + i * 1024);
        pu[i] = *(LAS float*)(size_t)(rdr + i * 1024 + FSW * 4);
        cgs[i] = *(LAS float*)(size_t)(rdr + i * 1024 + 512);
        cus[i] = *(LAS float*)(size_t)(rdr + i * 1024 + 512 + FSW * 4);
      }
      const float gg = (((pg[0] + pg[1]) + (pg[2] + pg[3])) - mb * ((cgs[0] + cgs[1]) + (cgs[2] + cgs[3]))) * ri;
      const float uu = (((pu[0] + pu[1]) + (pu[2] + pu[3])) - mb * ((cus[0] + cus[1]) + (cus[2] + cus[3]))) * ri;
      float a = gg * __builtin_amdgcn_rcpf(1.0f + __builtin_amdgcn_exp2f(-1.4426950408889634f * gg)) * uu;
      a = amask != 0.f ? a : 0.f;
      *(LAS u16*)(size_t)(aw) = (u16)(pk2(a, a) & 0xFFFFu);
      const v4u a0 = *(LAS v4u*)(size_t)(ar);
#pragma unroll
      for (int t = 0; t < 8; ++t) acc[t] = smf(a0, df[t], acc[t], sidq);
      SCHED();
      f32x4 o;
      QUADSUM(o, acc);
      stc16(rs, fo, v4u{nosent(asu(o[0])), nosent(asu(o[1])), nosent(asu(o[2])), nosent(asu(o[3]))});
    }
  } else {
    v4u gw[32];
    ARGS();
    SCHED();
    unsigned tg0 = (unsigned)__builtin_amdgcn_s_memrealtime();
    asm volatile("" : "+s"(tg0));
    __builtin_amdgcn_s_setprio(3);
    asm volatile("" : "+v"(tag), "+v"(taga));
    tag += 1u;
    taga += 1u;
    unsigned qd[16];
#pragma unroll
    for (int r = 0; r < 8; ++r) {
      const int f = ((r & 3) << 2) | ((2 * w4 + (r >> 2)) & 3);
      qd[r] = L_QA + (8 * w4 + r) * 512 + 16 * ((lane >> 1) ^ f) + 8 * (lane & 1);
      qd[8 + r] = qd[r] + 16384;
      asm volatile("" : "+v"(qd[r]), "+v"(qd[8 + r]));
    }
    unsigned base[2];
#pragma unroll
    for (int i = 0; i < 2; ++i) { base[i] = OFF_QA + (ba * NH + 8 * w4) * 2048 + lane * 16 + i * 1024; PIN(base[i]); }
    PV_ADDR();
#pragma nounroll
    for (int i = 0; i < 256 && (unsigned)__builtin_amdgcn_s_memrealtime() - tg0 < (unsigned)GMIN; ++i) __builtin_amdgcn_s_sleep(1);
    {
      v4u qa[16];
#pragma unroll
      for (int i = 0; i < 16; ++i) {
        if (i == 8) __builtin_amdgcn_s_setprio(2);
        qa[i] = __builtin_amdgcn_raw_buffer_load_b128(rs, base[i >> 3], (i & 7) * 2048, AUX_SC1);
        SCHED();
      }
      RAWBAR();
#define QA_HALF(hh, bad)                                                                             \
  _Pragma("unroll") for (int r = 0; r < 8; ++r) {                                                    \
    asm volatile("ds_write2_b32 %0, %1, %2 offset1:1" ::"v"(qd[8 * (hh) + r]), "v"(qa[8 * (hh) + r].x), "v"(qa[8 * (hh) + r].z) : "memory"); \
    bad |= (qa[8 * (hh) + r].y ^ taga) | (qa[8 * (hh) + r].w ^ taga);                                \
    PIN(bad);                                                                                        \
    SCHED();                                                                                         \
  }
      unsigned bad = 0;
      QA_HALF(0, bad);
      const bool late0 = !__all(bad == 0);
      if (!late0) BAR();
      QA_HALF(1, bad);
      if (__builtin_expect(!__all(bad == 0), 0)) {
        SPIN {
          FRESH();
#pragma unroll
          for (int i = 0; i < 16; ++i)
            if (((qa[i].y ^ taga) | (qa[i].w ^ taga)) != 0) qa[i] = __builtin_amdgcn_raw_buffer_load_b128(rs, base[i >> 3], (i & 7) * 2048, AUX_SC1);
          bad = 0;
          QA_HALF(0, bad);
          QA_HALF(1, bad);
          if (__all(bad == 0)) break;
        }
      }
      if (late0) BAR();
      BAR();
    }
    FTG(w4);
    __builtin_amdgcn_s_setprio(0);
    __builtin_amdgcn_s_sleep(CADELAY);
    if (w < 6) { LOAD_CA(); }
    SCHED();
    RAWBAR();
    if (w >= 6) { LOAD_CA(); }
    if (w == 4 && lane < 32) {
      const f32x4 m4 = *(LAS f32x4*)(L + L_MX + r32 * 16);
      stc8(rs, OFF_STAT + ((ba * 32 + sa_) * NH + lane) * 8, v2u{asu(fmaxf(fmaxf(m4[0], m4[1]), fmaxf(m4[2], m4[3]))), taga});
    }
    SCHED();
    BAR();
    pv_publish(pr, sw, sr, po);
    if (!ffn) return;
    BAR();
    const v2u xv = *(const GAS v2u*)(x + b * DM + 64 * w + 8 * ck + 4 * (q4 & 1));
    SCHED();
    if (w < 6) stc16(rs, OFF_FP + (b * 32 + s) * 2048 + w4 * 1024 + lane * 16, sent4);
#pragma unroll
    for (int i = 0; i < 32; ++i) GWLD(i);
    SCHED();
    H3_PREP();
    f32x4 acc[8];
    unsigned hr0 = L_HB + (g >> 3) * 1024 + (g & 4) * 128 + 128 * w4 + 32 * (((q4 >> 1) + b) & 3) + 16 * (q4 & 1);
    unsigned hr1 = L_HB + (g >> 3) * 1024 + (g & 4) * 128 + 128 * w4 + 32 * ((2 + (q4 >> 1) + b) & 3) + 16 * (q4 & 1);
    unsigned rdw = L_RED + ((w4 * 2 + (q4 >> 1)) * 128 + 8 * g + 4 * (q4 & 1)) * 4;
    PIN(hr0); PIN(hr1); PIN(rdw);
    SCHED();
    v8u gf[16];
    tr16(gw, gf);
    tr16(gw + 16, gf + 8);
#pragma unroll
    for (int i = 0; i < 16; ++i) asm volatile("" : "+v"(gf[i]));
    DRAIN();
    BAR();
    GATHER_COMBINE(obase, oh, xi, hbw, ZACC(acc), (void)0);
    BAR();
    {
      const v4u h0 = *(LAS v4u*)(size_t)(hr0), h1 = *(LAS v4u*)(size_t)(hr1);
#pragma unroll
      for (int t = 0; t < 8; ++t) acc[t] = smf(h0, gf[t], acc[t], sidq);
#pragma unroll
      for (int t = 0; t < 8; ++t) acc[t] = smf(h1, gf[8 + t], acc[t], sidq);
      SCHED();
      f32x4 o;
      QUADSUM(o, acc);
      *(LAS f32x4*)(size_t)(rdw) = o;
    }
    BAR();
    if (w == 5 && s == 16) {
      SPIN {
        FRESH();
        const unsigned t = ldc4(rs, OFF_SIG4 + (b * 32 + (r32 < NFS ? r32 : 0)) * 128);
        if (__all(t == tag)) break;
        __builtin_amdgcn_s_sleep(1);
      }
      stc4(rs, lane == 0 ? OFF_EPOCH + b * 4 : 0xFFFFFF00u, tag);
    }
    if (w == 4 && s < 16) {
      unsigned fb = OFF_FP + (b * 32 + rg) * 2048 + (32 * s + 4 * ck) * 4;
      unsigned fb3 = rg + 24 < NFS ? fb + 24 * 2048 : 0xFFFFFF00u;
      const unsigned hw = *(LAS unsigned*)(L + L_HB + hf * 512 + (8 * s + ck) * 4);
      float h0 = bflo(hw), h1 = bfhi(hw);
      PIN(fb); PIN(fb3); PIN(h0); PIN(h1);
      SPIN {
        FRESH();
        const unsigned t = ldc4(rs, OFF_SIG4 + (b * 32 + (r32 < NFS ? r32 : 0)) * 128);
        if (__all(t == tag)) break;
        __builtin_amdgcn_s_sleep(1);
      }
      __builtin_amdgcn_s_sleep(D4);
      f32x4 v;
      SPIN {
        FRESH();
        v4u fp[4];
#pragma unroll
        for (int i = 0; i < 3; ++i) fp[i] = __builtin_amdgcn_raw_buffer_load_b128(rs, fb, i * 8 * 2048, AUX_SC1);
        fp[3] = ldc16(rs, fb3);
        v = (__builtin_bit_cast(f32x4, fp[0]) + __builtin_bit_cast(f32x4, fp[1])) + (__builtin_bit_cast(f32x4, fp[2]) + __builtin_bit_cast(f32x4, fp[3]));
        const bool ok = umax(umax(mx4(fp[0]), mx4(fp[1])), umax(mx4(fp[2]), mx4(fp[3]))) != SENT;
        if (__all(ok)) break;
      }
      float v0 = swap32_add(v[0], v[2]), v1 = swap32_add(v[1], v[3]);
      PIN(v0); PIN(v1);
      v0 = swap16_add(v0, v0); PIN(v0);
      v1 = swap16_add(v1, v1); PIN(v1);
      v0 = addror<8>(v0, v0) + h0; PIN(v0);
      v1 = addror<8>(v1, v1) + h1; PIN(v1);
      if ((lane & 24) == 0) *(GAS unsigned*)(out + b * DM + 32 * s + 4 * ck + 2 * hf) = pk2(v0, v1);
    }
  }
}

extern "C" void kernel_launch(void* const* d_in, const int* in_sizes, int n_in, void* d_out, int out_size, void* d_ws, size_t ws_size,
                              hipStream_t stream) {
  (void)hipFuncSetAttribute((const void*)mega, hipFuncAttributeMaxDynamicSharedMemorySize, LDS_BYTES);
  mega<<<256, 512, LDS_BYTES, stream>>>((const u16*)d_in[0], (const u16*)d_in[1], (const u16*)d_in[2], (const u16*)d_in[3],
                                        (const u16*)d_in[4], (const u16*)d_in[5], (const u16*)d_in[7], (const u16*)d_in[8],
                                        (const u16*)d_in[9], (u16*)d_out, (unsigned char*)d_ws);
}
```
